```python
import torch

def _softmax(x, axis=-1):
    return torch.softmax(x, dim=axis)
def _gelu(x):
    return torch.nn.functional.gelu(x, approximate="tanh")
def _silu(x):
    return torch.nn.functional.silu(x)
def _sigmoid(x):
    return torch.sigmoid(x)
def _layernorm(x, axis=-1, eps=1e-5):
    m = x.mean(dim=axis, keepdim=True)
    v = x.var(dim=axis, keepdim=True, unbiased=False)
    return (x - m) / torch.sqrt(v + eps)
def _scatter_add(tgt, idx, src, axis=0):
    return tgt.clone().index_add_(axis, idx, src)
def _segment_sum(src, seg, n):
    out = torch.zeros((n,) + src.shape[1:], dtype=src.dtype)
    return out.index_add_(0, seg, src)
def _pad(x, pw):
    flat = tuple(v for pair in reversed(pw) for v in pair)
    return torch.nn.functional.pad(x, flat)

kernel_name = 'concat_heads__ssm_conv_s5__linear_attn_block__prompt'

def setup_inputs():

    _w = (lambda a: (lambda t: t.to(torch.bfloat16) if t.is_floating_point() else t)(torch.as_tensor(a)))
    def _A_setup():
        B, S, D = 1, 2048, 512
        P = D
        Lambda_re = -0.5 * (lambda _x: torch.exp(torch.as_tensor(_x)))((lambda *s: torch.randn(*s, dtype=torch.bfloat16) * 0.2)(P))
        Lambda_im = (lambda *s: torch.randn(*s, dtype=torch.bfloat16) * 0.8)(P) * 1.0
        B_re = (lambda *s: torch.randn(*s, dtype=torch.bfloat16) * 0.2)(P, D) * (D ** -0.5)
        B_im = (lambda *s: torch.randn(*s, dtype=torch.bfloat16) * 0.2)(P, D) * (D ** -0.5)
        C_re = (lambda *s: torch.randn(*s, dtype=torch.bfloat16) * 0.2)(D, P) * (P ** -0.5)
        C_im = (lambda *s: torch.randn(*s, dtype=torch.bfloat16) * 0.2)(D, P) * (P ** -0.5)
        log_dt = (lambda *s: torch.randn(*s, dtype=torch.bfloat16) * 0.2)(P) * 0.3 - 2.0
        Dskip = (lambda *s: torch.randn(*s, dtype=torch.bfloat16) * 0.05)(D)
        return {
            "x": _w((lambda *s: torch.randn(*s, dtype=torch.bfloat16) * 0.05)(B, S, D)),
            "Lambda_re": _w(Lambda_re),
            "Lambda_im": _w(Lambda_im),
            "B_re": _w(B_re),
            "B_im": _w(B_im),
            "C_re": _w(C_re),
            "C_im": _w(C_im),
            "log_dt": _w(log_dt),
            "Dskip": _w(Dskip),
        }
    _A_in = _A_setup()

    def _B_setup():
        B, S, D = 1, 2048, 512
        return {
            "x": _w((lambda *s: torch.randn(*s, dtype=torch.bfloat16) * 0.05)(B, S, D)),
            "wqkv": _w((lambda *s: torch.randn(*s, dtype=torch.bfloat16) * 0.4)(D, 3 * D)),
            "wo": _w((lambda *s: torch.randn(*s, dtype=torch.bfloat16) * 0.05)(D, D)),
        }
    _B_in = _B_setup()

    return {
        "A_x": _A_in["x"],
        "A_Lambda_re": _A_in["Lambda_re"],
        "A_Lambda_im": _A_in["Lambda_im"],
        "A_B_re": _A_in["B_re"],
        "A_B_im": _A_in["B_im"],
        "A_C_re": _A_in["C_re"],
        "A_C_im": _A_in["C_im"],
        "A_log_dt": _A_in["log_dt"],
        "A_Dskip": _A_in["Dskip"],
        "B_x": _B_in["x"],
        "B_wqkv": _B_in["wqkv"],
        "B_wo": _B_in["wo"],
    }

def reference(A_x, A_Lambda_re, A_Lambda_im, A_B_re, A_B_im, A_C_re, A_C_im, A_log_dt, A_Dskip, B_x, B_wqkv, B_wo):
    def _A_ref(x, Lambda_re, Lambda_im, B_re, B_im, C_re, C_im, log_dt, Dskip):
        Bsz, L, H = x.shape
        Lam = Lambda_re + 1j * Lambda_im
        Bmat = B_re + 1j * B_im
        Cmat = C_re + 1j * C_im
        dt = torch.exp(log_dt)
        Lam_bar = torch.exp(dt * Lam)
        B_bar = ((Lam_bar - 1.0) / Lam)[:, None] * Bmat
        Bu = torch.einsum("bld,pd->blp", x + 0j, B_bar)
        h = torch.zeros_like(Bu[:, 0, :])
        hs = []
        for t in range(L):
            h = Lam_bar[None, :] * h + Bu[:, t, :]
            hs.append(h)
        hseq = torch.stack(hs, 1)
        y = 2.0 * torch.einsum("blp,dp->bld", hseq, Cmat).real + x * Dskip[None, None, :]
        return y

    def _B_ref(x, wqkv, wo):
        B, S, D = x.shape
        H = 32; Dh = D // H
        qkv = (lambda x, *d: x.permute(*d))((x @ wqkv).reshape(B, S, 3, H, Dh), 2, 0, 3, 1, 4)
        q, k, v = qkv[0], qkv[1], qkv[2]
        phi = lambda t: torch.where(t > 0, t + 1, torch.exp(t)) + 1e-6
        pq, pk = phi(q), phi(k)
        kv = torch.einsum("bhsd,bhse->bhde", pk, v)
        z = torch.einsum("bhsd->bhd", pk)
        num = torch.einsum("bhqd,bhde->bhqe", pq, kv)
        den = torch.einsum("bhqd,bhd->bhq", pq, z)[..., None] + 1e-6
        o = num / den
        return (lambda x, *d: x.permute(*d))(o, 0, 2, 1, 3).reshape(B, S, D) @ wo

    a = _A_ref(A_x, A_Lambda_re, A_Lambda_im, A_B_re, A_B_im, A_C_re, A_C_im, A_log_dt, A_Dskip)
    b = _B_ref(B_x, B_wqkv, B_wo)
    return torch.concatenate([a, b], axis=-1)
```

```cpp
#include <hip/hip_runtime.h>
#include <hip/hip_bf16.h>
#include <stdint.h>
#include <utility>

typedef unsigned short u16;
typedef unsigned long long u64;
typedef short bf16x8 __attribute__((ext_vector_type(8)));
typedef short s16x4 __attribute__((ext_vector_type(4)));
typedef float f32x4 __attribute__((ext_vector_type(4)));
typedef float f32x16 __attribute__((ext_vector_type(16)));
typedef float f32x2 __attribute__((ext_vector_type(2)));
typedef __bf16 bfx2 __attribute__((ext_vector_type(2)));
typedef unsigned u32x4 __attribute__((ext_vector_type(4)));
typedef unsigned u32x2 __attribute__((ext_vector_type(2)));
#define LAS __attribute__((address_space(3)))
#define GAS __attribute__((address_space(1)))
#define CAS __attribute__((address_space(4)))
#define DI __device__ __forceinline__

constexpr unsigned WS_EP  = 0;
constexpr unsigned WS_FH  = 4096;
constexpr unsigned WS_FQ  = 8192;
constexpr unsigned WS_FKV = 12288;
constexpr unsigned WS_AGG = 65536;
constexpr unsigned WS_KVF = 262144;
constexpr unsigned WS_HF  = 1u << 20;
constexpr unsigned WS_PQ  = 5u << 20;
constexpr unsigned WS_KVS = 9u << 20;
constexpr unsigned WS_SPAN = 16u << 20;
constexpr unsigned SPIN_CAP = 1u << 20;

#define STAMP(kid, i) do {} while (0)
constexpr int LDS_BYTES = 163840;

DI float bfr(float x) { __bf16 b = (__bf16)x; return (float)b; }
DI unsigned pk2(float lo, float hi) { f32x2 f = {lo, hi}; bfx2 b = __builtin_convertvector(f, bfx2); return __builtin_bit_cast(unsigned, b); }
DI float phi1(float t) { const float r = t > 0.f ? bfr(t + 1.f) : bfr(__expf(t)); return r + 1e-6f; }
DI unsigned phi2(float a0, float a1) {
  const unsigned tb = pk2(a0, a1);
  return pk2(phi1(__uint_as_float(tb << 16)), phi1(__uint_as_float(tb & 0xffff0000u)));
}
#define SB0 __builtin_amdgcn_sched_barrier(0)
#define BAR() do { __builtin_amdgcn_s_barrier(); asm volatile("" ::: "memory"); } while (0)
#define MFMA16(a, b, c) __builtin_amdgcn_mfma_f32_16x16x32_bf16((a), (b), (c), 0, 0, 0)
#define MFMA32(a, b, c) __builtin_amdgcn_mfma_f32_32x32x16_bf16((a), (b), (c), 0, 0, 0)
DI void glds16(const void* g, char* l) { __builtin_amdgcn_global_load_lds((const GAS unsigned*)g, (LAS unsigned*)l, 16, 0, 0); }
DI s16x4 tr_read(const char* l) { return __builtin_amdgcn_ds_read_tr16_b64_v4i16((LAS s16x4*)l); }
DI __amdgpu_buffer_rsrc_t mk_rsrc(void* base, unsigned bytes) { return __builtin_amdgcn_make_buffer_rsrc(base, 0, bytes, 0x00020000); }
DI void st16(__amdgpu_buffer_rsrc_t r, unsigned off, u32x4 v) { __builtin_amdgcn_raw_buffer_store_b128(v, r, off, 0, 16); }
DI void st8(__amdgpu_buffer_rsrc_t r, unsigned off, u32x2 v) { __builtin_amdgcn_raw_buffer_store_b64(v, r, off, 0, 16); }
DI u32x4 ld16c(__amdgpu_buffer_rsrc_t r, unsigned off) { return __builtin_amdgcn_raw_buffer_load_b128(r, off, 0, 16); }
DI void cmul(float ar, float ai, float br, float bi, float& cr, float& ci) { cr = ar * br - ai * bi; ci = ar * bi + ai * br; }
template <int N> DI void wait_vm() { asm volatile("s_waitcnt vmcnt(%0)" :: "i"(N) : "memory"); }
template <int N, class T> DI void wait_vm_for(T& x) { asm volatile("s_waitcnt vmcnt(%1)" : "+v"(x) : "i"(N) : "memory"); }
template <int OFF> DI void rd128(bf16x8& d, unsigned a) { asm volatile("ds_read_b128 %0, %1 offset:%2" : "=v"(d) : "v"(a), "i"(OFF) : "memory"); }
template <int OFF> DI void rdtr(s16x4& d, unsigned a) { asm volatile("ds_read_b64_tr_b16 %0, %1 offset:%2" : "=v"(d) : "v"(a), "i"(OFF) : "memory"); }
template <int OFF, class T> DI void gld16c(T& d, const void* p) { asm volatile("global_load_dwordx4 %0, %1, off offset:%2 sc1" : "=v"(d) : "v"(p), "i"(OFF) : "memory"); }
template <class T> DI void pin(T& x) { asm volatile("" : "+v"(x) :: "memory"); }
DI void wait_lgkm0() { asm volatile("s_waitcnt lgkmcnt(0)" ::: "memory"); }
template <int... I, class F> DI void sfor_impl(std::integer_sequence<int, I...>, F&& f) { (f(std::integral_constant<int, I>{}), ...); }
template <int N, class F> DI void sfor(F&& f) { sfor_impl(std::make_integer_sequence<int, N>{}, f); }
DI void wait_flags32(const char* flags, unsigned tag, int lane) {
  GAS u64* f = (GAS u64*)flags + (lane & 31);
  const u64 want = ((u64)tag << 32) | (u64)(~tag);
  for (unsigned spins = 0; spins < SPIN_CAP; ++spins) {
    const u64 v = __hip_atomic_load(f, __ATOMIC_RELAXED, __HIP_MEMORY_SCOPE_AGENT);
    if (__all(v == want)) break;
    __builtin_amdgcn_s_sleep(4);
  }
  asm volatile("" ::: "memory");
}
DI void set_flag(char* flag, unsigned tag, int lane) {
  wait_vm<0>();
  if (lane == 0) __hip_atomic_store((GAS u64*)flag, ((u64)tag << 32) | (u64)(~tag), __ATOMIC_RELAXED, __HIP_MEMORY_SCOPE_AGENT);
}

struct Args {
  const u16 *Ax, *Lre, *Lim, *Bre, *Bim, *Cre, *Cim, *logdt, *Dsk, *Bx, *wqkv, *wo;
  char* ws;
  float* out;
};

__global__ __launch_bounds__(512, 2) void fused(Args a) {
  extern __shared__ __attribute__((aligned(16))) char smem[];
  const int tid = threadIdx.x, lane = tid & 63;
  const int w8 = __builtin_amdgcn_readfirstlane(tid >> 6), w = w8 & 3, kh = w8 >> 2;
  const int b = blockIdx.x, xcd = b & 7, idx = b >> 3, j = idx & 15;
  const int rb = 4 * (xcd >> 1) + (j >> 2), cg = 4 * (xcd & 1) + (j & 3);
  const int l15 = lane & 15, g = lane >> 4, l31 = lane & 31, hh = lane >> 5;
  const int sw = (l15 >> 1) & 7;
  const unsigned lds0 = (unsigned)(size_t)smem;
  const __amdgpu_buffer_rsrc_t wsr = mk_rsrc(a.ws, WS_SPAN);
  const bool isA = idx < 16;
  const unsigned tag = *(const CAS unsigned*)(a.ws + WS_EP + (isA ? 0 : 256)) + 1u;
  asm volatile("" :: "s"(tag));
  STAMP(0, 0);
  if (w8 >= 4) {
    const u16* src[10];
    const int rot = (rb + cg) & 7;
    if (isA) {
#pragma unroll
      for (int i = 0; i < 4; ++i) {
        const int rho = (4 * w + i) * 8 + (lane >> 3);
        const int ch = ((lane & 7) ^ ((rho >> 1) & 7)) * 8;
        const int ii = rho & 15, m = rho >> 4;
        const int t = 32 * (ii >> 2) + 16 * (ii & 1) + 2 * m + ((ii >> 1) & 1);
        src[i] = a.Ax + (size_t)(rb * 128 + t) * 512 + ch;
        src[4 + i] = ((rho >> 6) ? a.Bim : a.Bre) + (size_t)(cg * 64 + (rho & 63)) * 512 + ch;
      }
      auto issue = [&](int s) {
#pragma unroll
        for (int i = 0; i < 8; ++i) glds16(src[i] + ((s + rot) & 7) * 64, smem + (s & 3) * 32768 + (i >> 2) * 16384 + w * 4096 + (i & 3) * 1024);
      };
      issue(0); issue(1);
      sfor<8>([&](auto kc) {
        constexpr int k = decltype(kc)::value;
        wait_vm<(k == 0 ? 8 : k <= 5 ? 16 : k == 6 ? 8 : 0)>();
        BAR();
        if constexpr (k == 0) issue(2);
        if constexpr (k + 3 < 8) issue(k + 3);
      });
      const u16* cs[2];
#pragma unroll
      for (int i = 0; i < 2; ++i) {
        const int rho = (2 * w + i) * 8 + (lane >> 3);
        cs[i] = a.Cre + (size_t)(cg * 64 + rho) * 512 + ((lane & 7) ^ ((rho >> 1) & 7)) * 8;
      }
      const ptrdiff_t imo = a.Cim - a.Cre;
#pragma unroll
      for (int sg = 0; sg < 16; ++sg) {
        if (sg == 12) BAR();
#pragma unroll
        for (int i = 0; i < 2; ++i) glds16(cs[i] + (sg >> 3) * imo + (sg & 7) * 64, smem + sg * 8192 + (2 * w + i) * 1024);
      }
    } else {
#pragma unroll
      for (int i = 0; i < 4; ++i) {
        const int rho = (4 * w + i) * 8 + (lane >> 3);
        src[i] = a.Bx + (size_t)(rb * 128 + rho) * 512 + ((lane & 7) ^ ((rho >> 1) & 7)) * 8;
      }
#pragma unroll
      for (int i = 0; i < 6; ++i) {
        const int jj = w * 6 + i, part = jj >> 3;
        const int sidx = (jj & 7) * 8 + (lane >> 3);
        const int k = ((sidx >> 4) << 4) | ((sidx & 1) << 3) | ((sidx >> 1) & 7);
        const int pos = lane & 7;
        const int cc = (((pos >> 1) ^ (k & 3)) << 1) | (pos & 1);
        src[4 + i] = a.wqkv + (size_t)k * 1536 + part * 512 + cg * 64 + cc * 8;
      }
      auto issue = [&](int s) {
#pragma unroll
        for (int i = 0; i < 4; ++i) glds16(src[i] + ((s + rot) & 7) * 64, smem + (s & 3) * 40960 + w * 4096 + i * 1024);
#pragma unroll
        for (int i = 0; i < 6; ++i) glds16(src[4 + i] + ((s + rot) & 7) * 64 * 1536, smem + (s & 3) * 40960 + 16384 + w * 6144 + i * 1024);
      };
      issue(0); issue(1);
      sfor<8>([&](auto kc) {
        constexpr int k = decltype(kc)::value;
        wait_vm<(k == 0 ? 10 : k <= 5 ? 20 : k == 6 ? 10 : 0)>();
        BAR();
        if constexpr (k == 0) issue(2);
        if constexpr (k + 3 < 8) issue(k + 3);
      });
      const int k0 = w * 8 + (lane >> 3);
      const u16* os = a.wo + (size_t)k0 * 512 + cg * 64 + ((lane & 7) ^ (((k0 >> 1) & 1) << 2)) * 8;
#pragma unroll
      for (int i = 0; i < 16; ++i) glds16(os + (size_t)i * 32 * 512, smem + (i * 4 + w) * 1024);
    }
    wait_vm<0>();
  } else if (isA) {
    const int p = cg * 64 + w * 16 + l15;
    const float la = __uint_as_float((unsigned)a.Lre[p] << 16), lb = __uint_as_float((unsigned)a.Lim[p] << 16);
    const float dt = bfr(expf(__uint_as_float((unsigned)a.logdt[p] << 16)));
    float Lr, Li;
    { const float er = expf(dt * la); float sn, cs; sincosf(dt * lb, &sn, &cs); Lr = er * cs; Li = er * sn; }
    float c2r, c2i;
    { const float nr = Lr - 1.f, ni = Li, den = la * la + lb * lb;
      c2r = 2.f * ((nr * la + ni * lb) / den); c2i = 2.f * ((ni * la - nr * lb) / den); }
    float L16r, L16i, L32r, L32i, L64r, L64i, L128r, L128i, L512r, L512i, Lgr, Lgi;
    { float tr, ti, ur, ui;
      cmul(Lr, Li, Lr, Li, tr, ti); cmul(tr, ti, tr, ti, ur, ui); cmul(ur, ui, ur, ui, tr, ti); cmul(tr, ti, tr, ti, L16r, L16i);
      cmul(L16r, L16i, L16r, L16i, L32r, L32i); cmul(L32r, L32i, L32r, L32i, L64r, L64i);
      cmul(L64r, L64i, L64r, L64i, L128r, L128i); cmul(L128r, L128i, L128r, L128i, tr, ti); cmul(tr, ti, tr, ti, L512r, L512i);
      cmul(L32r, L32i, L64r, L64i, tr, ti);
      Lgr = g == 0 ? 1.f : g == 1 ? L32r : g == 2 ? L64r : tr;
      Lgi = g == 0 ? 0.f : g == 1 ? L32i : g == 2 ? L64i : ti; }
    asm volatile("" : "+v"(Lr), "+v"(Li), "+v"(c2r), "+v"(c2i), "+v"(L16r), "+v"(L16i), "+v"(L32r), "+v"(L32i), "+v"(L64r), "+v"(L64i),
                 "+v"(L128r), "+v"(L128i), "+v"(L512r), "+v"(L512i), "+v"(Lgr), "+v"(Lgi));
    f32x4 acc[8][2];
#pragma unroll
    for (int m = 0; m < 8; ++m) { acc[m][0] = (f32x4){0.f, 0.f, 0.f, 0.f}; acc[m][1] = (f32x4){0.f, 0.f, 0.f, 0.f}; }
    const unsigned xoff = lds0 + l15 * 128 + ((g ^ sw) * 16);
    const unsigned woff = lds0 + 16384 + (w * 16 + l15) * 128 + ((g ^ sw) * 16);
    bf16x8 X[2][8], W[2][2];
    auto rdA = [&](auto rc, auto hc) {
      constexpr int r = decltype(rc)::value, hs = decltype(hc)::value, s = hs >> 1, kk = hs & 1;
      if constexpr (r < 2) rd128<r * 8192>(W[kk][r], (woff + (s & 3) * 32768) ^ (kk * 64));
      else rd128<(r - 2) * 2048>(X[kk][r - 2], (xoff + (s & 3) * 32768) ^ (kk * 64));
    };
    BAR();
    sfor<10>([&](auto rc) { rdA(rc, std::integral_constant<int, 0>{}); });
    wait_lgkm0();
    sfor<16>([&](auto hc) {
      constexpr int hs = decltype(hc)::value, u = hs & 1;
      sfor<8>([&](auto i) { pin(X[u][decltype(i)::value]); });
      sfor<2>([&](auto i) { pin(W[u][decltype(i)::value]); });
      if constexpr (u == 1) BAR();
      sfor<4>([&](auto gc) {
        constexpr int G = decltype(gc)::value;
        if constexpr (hs < 15) {
          constexpr int r0 = G < 2 ? 3 * G : 6 + 2 * (G - 2), nr = G < 2 ? 3 : 2;
          sfor<nr>([&](auto rr) { rdA(std::integral_constant<int, r0 + decltype(rr)::value>{}, std::integral_constant<int, hs + 1>{}); });
        }
#pragma unroll
        for (int mm = 0; mm < 2; ++mm) {
          acc[2 * G + mm][0] = MFMA16(X[u][2 * G + mm], W[u][0], acc[2 * G + mm][0]);
          acc[2 * G + mm][1] = MFMA16(X[u][2 * G + mm], W[u][1], acc[2 * G + mm][1]);
        }
        SB0;
      });
      if constexpr (hs < 15) wait_lgkm0();
    });
    STAMP(0, 1);
    const f32x2 LR = {Lr, Lr}, LI = {Li, Li}, NLI = {-Li, -Li};
    f32x2 AR = {0.f, 0.f}, AI = {0.f, 0.f};
#pragma unroll
    for (int m = 0; m < 8; ++m)
#pragma unroll
      for (int r2 = 0; r2 < 2; ++r2) {
        const f32x2 UR = {acc[m][0][2 * r2], acc[m][0][2 * r2 + 1]}, UI = {acc[m][1][2 * r2], acc[m][1][2 * r2 + 1]};
        const f32x2 nr = __builtin_elementwise_fma(LR, AR, __builtin_elementwise_fma(NLI, AI, UR));
        const f32x2 ni = __builtin_elementwise_fma(LR, AI, __builtin_elementwise_fma(LI, AR, UI));
        AR = nr; AI = ni;
      }
    float ar, ai;
    cmul(L16r, L16i, AR[0], AI[0], ar, ai); ar += AR[1]; ai += AI[1];
    {
      float yr = __shfl_up(ar, 16), yi = __shfl_up(ai, 16), tr, ti;
      cmul(L32r, L32i, yr, yi, tr, ti);
      if (g >= 1) { ar += tr; ai += ti; }
      yr = __shfl_up(ar, 32); yi = __shfl_up(ai, 32);
      cmul(L64r, L64i, yr, yi, tr, ti);
      if (g >= 2) { ar += tr; ai += ti; }
    }
    if (g == 3) st16(wsr, WS_AGG + (rb * 512 + p) * 16, (u32x4){__float_as_uint(ar), tag, __float_as_uint(ai), tag});
    float er_ = __shfl_up(ar, 16), ei_ = __shfl_up(ai, 16);
    if (g == 0) { er_ = 0.f; ei_ = 0.f; }
    STAMP(0, 2);
    if (rb > 0) {
      u32x4 v[4];
      const int j0 = rb - 1 - 4 * g;
      for (unsigned spins = 0; spins < SPIN_CAP; ++spins) {
        bool ok = true;
        asm volatile("" ::: "memory");
#pragma unroll
        for (int i = 0; i < 4; ++i) {
          const int jj = j0 - i;
          v[i] = ld16c(wsr, WS_AGG + ((jj < 0 ? 0 : jj) * 512 + p) * 16);
          ok &= (jj < 0) || (v[i][1] == tag && v[i][3] == tag);
        }
        if (__all(ok)) break;
        __builtin_amdgcn_s_sleep(1);
      }
      float qr = 0.f, qi = 0.f;
#pragma unroll
      for (int i = 3; i >= 0; --i) {
        const float vr = __uint_as_float(v[i][0]), vi = __uint_as_float(v[i][2]);
        const bool valid = (j0 - i) >= 0, first = (i == 3 || (j0 - i - 1) < 0);
        const float nr = fmaf(L128r, qr, fmaf(-L128i, qi, vr)), ni = fmaf(L128r, qi, fmaf(L128i, qr, vi));
        qr = valid ? (first ? vr : nr) : 0.f; qi = valid ? (first ? vi : ni) : 0.f;
      }
      const float q0r = __shfl(qr, l15), q0i = __shfl(qi, l15), q1r = __shfl(qr, l15 + 16), q1i = __shfl(qi, l15 + 16);
      const float q2r = __shfl(qr, l15 + 32), q2i = __shfl(qi, l15 + 32), q3r = __shfl(qr, l15 + 48), q3i = __shfl(qi, l15 + 48);
      float tr = q3r, ti = q3i, ur, ui;
      if (rb > 12) { cmul(L512r, L512i, tr, ti, ur, ui); tr = q2r + ur; ti = q2i + ui; } else { tr = q2r; ti = q2i; }
      if (rb > 8)  { cmul(L512r, L512i, tr, ti, ur, ui); tr = q1r + ur; ti = q1i + ui; } else { tr = q1r; ti = q1i; }
      if (rb > 4)  { cmul(L512r, L512i, tr, ti, ur, ui); tr = q0r + ur; ti = q0i + ui; } else { tr = q0r; ti = q0i; }
      cmul(Lgr, Lgi, tr, ti, ur, ui);
      er_ += ur; ei_ += ui;
    }
    STAMP(0, 4);
    f32x2 HR, HI;
    { float tr, ti; cmul(L16r, L16i, er_, ei_, tr, ti); HR = (f32x2){er_, tr + AR[0]}; HI = (f32x2){ei_, ti + AI[0]}; }
    const f32x2 CR = {c2r, c2r}, NCI = {-c2i, -c2i}, NCR = {-c2r, -c2r};
    u32x4 o_re[2][2], o_im[2][2];
#pragma unroll
    for (int m = 0; m < 8; ++m) {
      f32x2 ore[2], oim[2];
#pragma unroll
      for (int r2 = 0; r2 < 2; ++r2) {
        const f32x2 UR = {acc[m][0][2 * r2], acc[m][0][2 * r2 + 1]}, UI = {acc[m][1][2 * r2], acc[m][1][2 * r2 + 1]};
        const f32x2 nr = __builtin_elementwise_fma(LR, HR, __builtin_elementwise_fma(NLI, HI, UR));
        const f32x2 ni = __builtin_elementwise_fma(LR, HI, __builtin_elementwise_fma(LI, HR, UI));
        HR = nr; HI = ni;
        ore[r2] = __builtin_elementwise_fma(CR, HR, NCI * HI);
        oim[r2] = __builtin_elementwise_fma(NCR, HI, NCI * HR);
      }
      o_re[0][m >> 2][m & 3] = pk2(ore[0][0], ore[1][0]); o_re[1][m >> 2][m & 3] = pk2(ore[0][1], ore[1][1]);
      o_im[0][m >> 2][m & 3] = pk2(oim[0][0], oim[1][0]); o_im[1][m >> 2][m & 3] = pk2(oim[0][1], oim[1][1]);
    }
    char* tb = smem + 131072 + w * 8192;
#pragma unroll
    for (int c = 0; c < 2; ++c)
#pragma unroll
      for (int hf = 0; hf < 2; ++hf) {
        *(u32x4*)(tb + l15 * 256 + ((g ^ (l15 & 3)) * 64) + c * 32 + hf * 16) = o_re[c][hf];
        *(u32x4*)(tb + 4096 + l15 * 256 + ((g ^ (l15 & 3)) * 64) + c * 32 + hf * 16) = o_im[c][hf];
      }
    __builtin_amdgcn_fence(__ATOMIC_RELEASE, "wavefront");
    __builtin_amdgcn_wave_barrier();
    {
      const int q = (lane >> 2) & 3, pp = lane & 3;
      const char* rbase = tb + (8 * hh + q) * 256 + (16 * (g & 1) + 4 * pp) * 2;
#pragma unroll
      for (int part = 0; part < 2; ++part)
#pragma unroll
        for (int tt = 0; tt < 4; ++tt) {
          const s16x4 lo = tr_read(rbase + part * 4096 + ((tt ^ q) * 64)), hi4 = tr_read(rbase + part * 4096 + ((tt ^ q) * 64) + 4 * 256);
          const bf16x8 f = __builtin_shufflevector(lo, hi4, 0, 1, 2, 3, 4, 5, 6, 7);
          st16(wsr, WS_HF + (((rb * 4 + tt) * 64 + part * 32 + cg * 4 + w) * 64 + lane) * 16, __builtin_bit_cast(u32x4, f));
        }
    }
    set_flag(a.ws + WS_FH + (rb * 32 + cg * 4 + w) * 8, tag, lane);
    STAMP(0, 5);
  } else {
    f32x4 aq[8], ak[8], av[8];
#pragma unroll
    for (int m = 0; m < 8; ++m) { aq[m] = (f32x4){0.f, 0.f, 0.f, 0.f}; ak[m] = aq[m]; av[m] = aq[m]; }
    const unsigned xoff = lds0 + l15 * 128 + ((g ^ sw) * 16);
    const int q = (lane >> 2) & 3, pp = lane & 3;
    const unsigned woff = lds0 + 16384 + ((((g >> 1) * 8 + q) * 2 + (g & 1)) * 128) + ((w ^ q) * 32) + pp * 8;
    bf16x8 X[2][8]; s16x4 T[2][6];
    auto rdB = [&](auto rc, auto hc) {
      constexpr int r = decltype(rc)::value, hs = decltype(hc)::value, s = hs >> 1, kk = hs & 1;
      if constexpr (r < 6) rdtr<(r >> 1) * 8192 + kk * 4096 + (r & 1) * 1024>(T[kk][r], woff + (s & 3) * 40960);
      else rd128<(r - 6) * 2048>(X[kk][r - 6], (xoff + (s & 3) * 40960) ^ (kk * 64));
    };
    BAR();
    sfor<14>([&](auto rc) { rdB(rc, std::integral_constant<int, 0>{}); });
    wait_lgkm0();
    sfor<16>([&](auto hc) {
      constexpr int hs = decltype(hc)::value, u = hs & 1;
      sfor<8>([&](auto i) { pin(X[u][decltype(i)::value]); });
      sfor<6>([&](auto i) { pin(T[u][decltype(i)::value]); });
      if constexpr (u == 1 && hs < 15) BAR();
      const bf16x8 wq = __builtin_shufflevector(T[u][0], T[u][1], 0, 1, 2, 3, 4, 5, 6, 7);
      const bf16x8 wk = __builtin_shufflevector(T[u][2], T[u][3], 0, 1, 2, 3, 4, 5, 6, 7);
      const bf16x8 wv = __builtin_shufflevector(T[u][4], T[u][5], 0, 1, 2, 3, 4, 5, 6, 7);
      sfor<8>([&](auto gc) {
        constexpr int m = decltype(gc)::value;
        if constexpr (hs < 15 && m < 7) {
          rdB(std::integral_constant<int, 2 * m>{}, std::integral_constant<int, hs + 1>{});
          rdB(std::integral_constant<int, 2 * m + 1>{}, std::integral_constant<int, hs + 1>{});
        }
        aq[m] = MFMA16(wq, X[u][m], aq[m]);
        ak[m] = MFMA16(X[u][m], wk, ak[m]);
        av[m] = MFMA16(X[u][m], wv, av[m]);
        SB0;
      });
      if constexpr (hs < 15) wait_lgkm0();
    });
    STAMP(0, 1);
    const int h = cg * 4 + w;
    f32x4 kvT = {0.f, 0.f, 0.f, 0.f}, zT = {0.f, 0.f, 0.f, 0.f};
    const bf16x8 ones = {0x3F80, 0x3F80, 0x3F80, 0x3F80, 0x3F80, 0x3F80, 0x3F80, 0x3F80};
#pragma unroll
    for (int s2 = 0; s2 < 4; ++s2) {
      u32x4 pkf, vf;
      pkf[0] = phi2(ak[2 * s2][0], ak[2 * s2][1]); pkf[1] = phi2(ak[2 * s2][2], ak[2 * s2][3]);
      pkf[2] = phi2(ak[2 * s2 + 1][0], ak[2 * s2 + 1][1]); pkf[3] = phi2(ak[2 * s2 + 1][2], ak[2 * s2 + 1][3]);
      vf[0] = pk2(av[2 * s2][0], av[2 * s2][1]); vf[1] = pk2(av[2 * s2][2], av[2 * s2][3]);
      vf[2] = pk2(av[2 * s2 + 1][0], av[2 * s2 + 1][1]); vf[3] = pk2(av[2 * s2 + 1][2], av[2 * s2 + 1][3]);
      const bf16x8 pb = __builtin_bit_cast(bf16x8, pkf), vb = __builtin_bit_cast(bf16x8, vf);
      kvT = MFMA16(vb, pb, kvT);
      zT = MFMA16(ones, pb, zT);
    }
    if (rb < 15) {
      const unsigned so = WS_KVS + ((rb * 32 + h) * 3 * 64 + lane) * 16;
      st16(wsr, so, (u32x4){__float_as_uint(kvT[0]), tag, __float_as_uint(kvT[1]), tag});
      st16(wsr, so + 1024, (u32x4){__float_as_uint(kvT[2]), tag, __float_as_uint(kvT[3]), tag});
      st16(wsr, so + 2048, (u32x4){__float_as_uint(zT[0]), tag, __float_as_uint(zT[0]), tag});
    }
    STAMP(0, 2);
#pragma unroll
    for (int m = 0; m < 8; ++m) {
      u32x2 o;
      o[0] = phi2(aq[m][0], aq[m][1]); o[1] = phi2(aq[m][2], aq[m][3]);
      const int tt = rb * 4 + (m >> 1), t31 = (m & 1) * 16 + l15;
      st8(wsr, WS_PQ + ((tt * 32 + h) * 64 + t31 + 32 * (g >> 1)) * 16 + (g & 1) * 8, o);
    }
    set_flag(a.ws + WS_FQ + (rb * 32 + h) * 8, tag, lane);
    STAMP(0, 4);
    if (rb == 15) {
      float sum[5] = {0.f, 0.f, 0.f, 0.f, 0.f};
      {
        u32x4 v[15][3];
        for (unsigned spins = 0; spins < SPIN_CAP; ++spins) {
          bool ok = true;
          asm volatile("" ::: "memory");
#pragma unroll
          for (int i = 0; i < 15; ++i)
#pragma unroll
            for (int c = 0; c < 3; ++c) v[i][c] = ld16c(wsr, WS_KVS + ((i * 32 + h) * 3 * 64 + c * 64 + lane) * 16);
          SB0;
#pragma unroll
          for (int i = 0; i < 15; ++i)
#pragma unroll
            for (int c = 0; c < 3; ++c) ok &= (v[i][c][1] == tag && v[i][c][3] == tag);
          if (__all(ok)) break;
          __builtin_amdgcn_s_sleep(1);
        }
#pragma unroll
        for (int i = 0; i < 15; ++i) {
          sum[0] += __uint_as_float(v[i][0][0]); sum[1] += __uint_as_float(v[i][0][2]);
          sum[2] += __uint_as_float(v[i][1][0]); sum[3] += __uint_as_float(v[i][1][2]);
          sum[4] += __uint_as_float(v[i][2][0]);
        }
      }
      sum[0] += kvT[0]; sum[1] += kvT[1]; sum[2] += kvT[2]; sum[3] += kvT[3]; sum[4] += zT[0];
      u16* tb = (u16*)(smem + 98304 + w * 1024);
#pragma unroll
      for (int r = 0; r < 4; ++r) tb[(4 * g + r) * 16 + l15] = (u16)(pk2(sum[r], 0.f) & 0xffffu);
      if (g == 0) tb[256 + l15] = (u16)(pk2(sum[4], 0.f) & 0xffffu);
      __builtin_amdgcn_fence(__ATOMIC_RELEASE, "wavefront");
      __builtin_amdgcn_wave_barrier();
      const u32x4 f = *(const u32x4*)(tb + (l31 < 16 ? l31 : 16) * 16 + hh * 8);
      st16(wsr, WS_KVF + (h * 64 + lane) * 16, f);
      set_flag(a.ws + WS_FKV + h * 8, tag, lane);
    }
    STAMP(0, 5);
  }

  const int d4 = lane & 15, r4 = lane >> 4;
  const __amdgpu_buffer_rsrc_t outr = mk_rsrc(a.out, 2048 * 1024 * 4);
  STAMP(1, 0);
  f32x16 acc[2];
#pragma unroll
  for (int r = 0; r < 16; ++r) { acc[0][r] = 0.f; acc[1][r] = 0.f; }
  u32x2 xv[4], dv;
  if (isA) {
    {
      const u16* xp = a.Ax + (size_t)(rb * 128 + w8 * 16 + r4) * 512 + cg * 64 + d4 * 4;
      asm volatile("global_load_dwordx2 %0, %5, off offset:-4096\n\tglobal_load_dwordx2 %1, %5, off\n\t"
                   "global_load_dwordx2 %2, %6, off offset:-4096\n\tglobal_load_dwordx2 %3, %6, off\n\t"
                   "global_load_dwordx2 %4, %7, off"
                   : "=&v"(xv[0]), "=&v"(xv[1]), "=&v"(xv[2]), "=&v"(xv[3]), "=&v"(dv)
                   : "v"(xp + 2048), "v"(xp + 2048 * 3), "v"(a.Dsk + cg * 64 + d4 * 4) : "memory");
    }
    if (w8 == 0) {
      wait_flags32(a.ws + WS_FH + rb * 256, tag, lane);
      if (b == 126 && lane == 0)
        __hip_atomic_store((GAS unsigned*)(a.ws + WS_EP), tag, __ATOMIC_RELAXED, __HIP_MEMORY_SCOPE_AGENT);
    }
    STAMP(1, 2);
    BAR();
    const char* hf = a.ws + WS_HF + (((size_t)(rb * 4 + w) * 64 + kh * 32) * 64 + lane) * 16;
    bf16x8 H[32];
    sfor<32>([&](auto i) { constexpr int k = decltype(i)::value; gld16c<(k & 3) * 1024>(H[k], hf + (k >> 2) * 4096); });
    STAMP(1, 3);
    unsigned cb[4];
#pragma unroll
    for (int i = 0; i < 4; ++i) cb[i] = lds0 + kh * 65536 + l31 * 128 + (((2 * i + hh) ^ ((l31 >> 1) & 7)) * 16);
    bf16x8 Cc[2][2];
    rd128<0>(Cc[0][0], cb[0]); rd128<4096>(Cc[0][1], cb[0]);
    sfor<32>([&](auto kc) {
      constexpr int k = decltype(kc)::value, u = k & 1;
      wait_vm_for<31 - k>(H[k]);
      if constexpr (k < 31) {
        rd128<((k + 1) >> 2) * 8192>(Cc[u ^ 1][0], cb[(k + 1) & 3]); rd128<((k + 1) >> 2) * 8192 + 4096>(Cc[u ^ 1][1], cb[(k + 1) & 3]);
        asm volatile("s_waitcnt lgkmcnt(2)" : "+v"(Cc[u][0]), "+v"(Cc[u][1]) :: "memory");
      } else {
        asm volatile("s_waitcnt lgkmcnt(0)" : "+v"(Cc[u][0]), "+v"(Cc[u][1]) :: "memory");
      }
      acc[0] = MFMA32(Cc[u][0], H[k], acc[0]);
      acc[1] = MFMA32(Cc[u][1], H[k], acc[1]);
      SB0;
    });
  } else {
    if (w8 == 0) wait_flags32(a.ws + WS_FQ + rb * 256, tag, lane);
    STAMP(1, 2);
    BAR();
    const char* pqf = a.ws + WS_PQ + (((size_t)(rb * 4 + w) * 32 + kh * 16) * 64 + lane) * 16;
    bf16x8 P[16];
    sfor<16>([&](auto i) { constexpr int k = decltype(i)::value; gld16c<(k & 3) * 1024>(P[k], pqf + (k >> 2) * 4096); });
    if (w8 == 4) {
      wait_flags32(a.ws + WS_FKV, tag, lane);
      if (b == 128 && lane == 0)
        __hip_atomic_store((GAS unsigned*)(a.ws + WS_EP + 256), tag, __ATOMIC_RELAXED, __HIP_MEMORY_SCOPE_AGENT);
    }
    BAR();
    STAMP(1, 4);
    {
      u32x4 kf[4];
      const char* kvf = a.ws + WS_KVF + (w8 * 4 * 64 + lane) * 16;
      sfor<4>([&](auto i) { constexpr int k = decltype(i)::value; gld16c<k * 1024>(kf[k], kvf); });
      wait_vm<0>();
      sfor<4>([&](auto i) { pin(kf[decltype(i)::value]); });
#pragma unroll
      for (int i = 0; i < 4; ++i) *(u32x4*)(smem + 65536 + (w8 * 4 + i) * 1024 + lane * 16) = kf[i];
    }
    wait_lgkm0();
    BAR();
    STAMP(1, 3);
    sfor<16>([&](auto i) { pin(P[decltype(i)::value]); });
    const int q = (lane >> 2) & 3, pp = lane & 3;
    const unsigned kb = lds0 + 65536 + kh * 16384 + lane * 16;
    const unsigned wb0 = lds0 + kh * 32768 + (4 * hh + q) * 128 + ((((g & 1) * 2 + (pp >> 1)) ^ ((q >> 1) << 2)) * 16) + (pp & 1) * 8;
    const unsigned wb1 = wb0 ^ 64u;
    bf16x8 Kf[2]; s16x4 Wt[2][4];
    f32x16 O[2];
    f32x16 z16;
#pragma unroll
    for (int r = 0; r < 16; ++r) z16[r] = 0.f;
    rd128<0>(Kf[0], kb);
    rdtr<0>(Wt[0][0], wb0); rdtr<1024>(Wt[0][1], wb0); rdtr<0>(Wt[0][2], wb1); rdtr<1024>(Wt[0][3], wb1);
    rd128<1024>(Kf[1], kb);
    asm volatile("s_waitcnt lgkmcnt(5)" : "+v"(Kf[0]) :: "memory");
    O[0] = MFMA32(Kf[0], P[0], z16);
    sfor<16>([&](auto hc) {
      constexpr int h = decltype(hc)::value, u = h & 1;
      if constexpr (h < 15) {
        asm volatile("s_waitcnt lgkmcnt(0)" : "+v"(Kf[u ^ 1]), "+v"(Wt[u][0]), "+v"(Wt[u][1]), "+v"(Wt[u][2]), "+v"(Wt[u][3]) :: "memory");
        O[u ^ 1] = MFMA32(Kf[u ^ 1], P[h + 1], z16);
        SB0;
        rdtr<(h + 1) * 2048>(Wt[u ^ 1][0], wb0); rdtr<(h + 1) * 2048 + 1024>(Wt[u ^ 1][1], wb0);
        rdtr<(h + 1) * 2048>(Wt[u ^ 1][2], wb1); rdtr<(h + 1) * 2048 + 1024>(Wt[u ^ 1][3], wb1);
        if constexpr (h < 14) rd128<(h + 2) * 1024>(Kf[u], kb);
      } else {
        asm volatile("s_waitcnt lgkmcnt(0)" : "+v"(Wt[u][0]), "+v"(Wt[u][1]), "+v"(Wt[u][2]), "+v"(Wt[u][3]) :: "memory");
      }
      const float den = bfr(bfr(O[u][8]) + 1e-6f);
      const float rc = __builtin_amdgcn_rcpf(den);
      u32x4 of;
#pragma unroll
      for (int i = 0; i < 4; ++i) of[i] = pk2(O[u][2 * i] * rc, O[u][2 * i + 1] * rc);
      const bf16x8 ob = __builtin_bit_cast(bf16x8, of);
      acc[0] = MFMA32(__builtin_shufflevector(Wt[u][0], Wt[u][1], 0, 1, 2, 3, 4, 5, 6, 7), ob, acc[0]);
      acc[1] = MFMA32(__builtin_shufflevector(Wt[u][2], Wt[u][3], 0, 1, 2, 3, 4, 5, 6, 7), ob, acc[1]);
      SB0;
    });
  }
  STAMP(1, 1);
  BAR();
  {
    char* tb = smem + kh * 32768 + w * 8192;
#pragma unroll
    for (int nt = 0; nt < 2; ++nt)
#pragma unroll
      for (int q = 0; q < 4; ++q)
        *(f32x4*)(tb + l31 * 256 + (((nt * 8 + 2 * q + hh) ^ (l31 & 15)) * 16)) =
            (f32x4){acc[nt][4 * q], acc[nt][4 * q + 1], acc[nt][4 * q + 2], acc[nt][4 * q + 3]};
  }
  wait_lgkm0();
  BAR();
  if (isA) { sfor<4>([&](auto i) { pin(xv[decltype(i)::value]); }); pin(dv); }
#pragma unroll
  for (int it = 0; it < 4; ++it) {
    const int row = w8 * 16 + it * 4 + r4;
    const char* sp = smem + row * 256 + ((d4 ^ (row & 15)) * 16);
    f32x4 s = *(const f32x4*)sp + *(const f32x4*)(sp + 32768);
    const unsigned oo = ((rb * 128 + row) * 1024 + cg * 64 + d4 * 4) * 4;
    if (isA) {
      s[0] += bfr(__uint_as_float(xv[it][0] << 16) * __uint_as_float(dv[0] << 16));
      s[1] += bfr(__uint_as_float(xv[it][0] & 0xffff0000u) * __uint_as_float(dv[0] & 0xffff0000u));
      s[2] += bfr(__uint_as_float(xv[it][1] << 16) * __uint_as_float(dv[1] << 16));
      s[3] += bfr(__uint_as_float(xv[it][1] & 0xffff0000u) * __uint_as_float(dv[1] & 0xffff0000u));
      st16(outr, oo, __builtin_bit_cast(u32x4, s));
    } else {
      s[0] = bfr(s[0]); s[1] = bfr(s[1]); s[2] = bfr(s[2]); s[3] = bfr(s[3]);
      st16(outr, oo + 2048, __builtin_bit_cast(u32x4, s));
    }
  }
  STAMP(1, 5);
}

extern "C" void kernel_launch(void* const* d_in, const int* in_sizes, int n_in, void* d_out, int out_size,
                              void* d_ws, size_t ws_size, hipStream_t stream) {
  Args a;
  a.Ax = (const u16*)d_in[0]; a.Lre = (const u16*)d_in[1]; a.Lim = (const u16*)d_in[2];
  a.Bre = (const u16*)d_in[3]; a.Bim = (const u16*)d_in[4]; a.Cre = (const u16*)d_in[5]; a.Cim = (const u16*)d_in[6];
  a.logdt = (const u16*)d_in[7]; a.Dsk = (const u16*)d_in[8]; a.Bx = (const u16*)d_in[9];
  a.wqkv = (const u16*)d_in[10]; a.wo = (const u16*)d_in[11];
  a.ws = (char*)d_ws; a.out = (float*)d_out;
  (void)hipFuncSetAttribute((const void*)fused, hipFuncAttributeMaxDynamicSharedMemorySize, LDS_BYTES);
  fused<<<256, 512, LDS_BYTES, stream>>>(a);
}
```

```cpp
#include <hip/hip_runtime.h>
#include <hip/hip_bf16.h>
#include <stdint.h>
#include <utility>

typedef unsigned short u16;
typedef unsigned long long u64;
typedef short bf16x8 __attribute__((ext_vector_type(8)));
typedef short s16x4 __attribute__((ext_vector_type(4)));
typedef float f32x4 __attribute__((ext_vector_type(4)));
typedef float f32x16 __attribute__((ext_vector_type(16)));
typedef float f32x2 __attribute__((ext_vector_type(2)));
typedef __bf16 bfx2 __attribute__((ext_vector_type(2)));
typedef unsigned u32x4 __attribute__((ext_vector_type(4)));
typedef unsigned u32x2 __attribute__((ext_vector_type(2)));
#define LAS __attribute__((address_space(3)))
#define GAS __attribute__((address_space(1)))
#define DI __device__ __forceinline__

constexpr unsigned WS_EP  = 0;
constexpr unsigned WS_FH  = 4096;
constexpr unsigned WS_FQ  = 8192;
constexpr unsigned WS_AGG = 65536;
constexpr unsigned WS_KVF = 262144;
constexpr unsigned WS_HF  = 1u << 20;
constexpr unsigned WS_PQ  = 5u << 20;
constexpr unsigned WS_KVS = 9u << 20;
constexpr unsigned WS_SPAN = 16u << 20;
constexpr unsigned SPIN_CAP = 1u << 20;

constexpr int LDS_BYTES = 163840;

DI float bfr(float x) { __bf16 b = (__bf16)x; return (float)b; }
DI unsigned pk2(float lo, float hi) { f32x2 f = {lo, hi}; bfx2 b = __builtin_convertvector(f, bfx2); return __builtin_bit_cast(unsigned, b); }
DI float phi1(float t) { const float r = t > 0.f ? bfr(t + 1.f) : bfr(__expf(t)); return r + 1e-6f; }
DI unsigned phi2(float a0, float a1) {
  const unsigned tb = pk2(a0, a1);
  return pk2(phi1(__uint_as_float(tb << 16)), phi1(__uint_as_float(tb & 0xffff0000u)));
}
#define SB0 __builtin_amdgcn_sched_barrier(0)
#define BAR() do { __builtin_amdgcn_s_barrier(); asm volatile("" ::: "memory"); } while (0)
#define MFMA16(a, b, c) __builtin_amdgcn_mfma_f32_16x16x32_bf16((a), (b), (c), 0, 0, 0)
#define MFMA32(a, b, c) __builtin_amdgcn_mfma_f32_32x32x16_bf16((a), (b), (c), 0, 0, 0)
DI void glds16(const void* g, char* l) { __builtin_amdgcn_global_load_lds((const GAS unsigned*)g, (LAS unsigned*)l, 16, 0, 0); }
DI s16x4 tr_read(const char* l) { return __builtin_amdgcn_ds_read_tr16_b64_v4i16((LAS s16x4*)l); }
DI __amdgpu_buffer_rsrc_t mk_rsrc(void* base, unsigned bytes) { return __builtin_amdgcn_make_buffer_rsrc(base, 0, bytes, 0x00020000); }
DI void st16(__amdgpu_buffer_rsrc_t r, unsigned off, u32x4 v) { __builtin_amdgcn_raw_buffer_store_b128(v, r, off, 0, 16); }
DI void st8(__amdgpu_buffer_rsrc_t r, unsigned off, u32x2 v) { __builtin_amdgcn_raw_buffer_store_b64(v, r, off, 0, 16); }
DI u32x4 ld16c(__amdgpu_buffer_rsrc_t r, unsigned off) { return __builtin_amdgcn_raw_buffer_load_b128(r, off, 0, 16); }
DI u32x2 ld8c(__amdgpu_buffer_rsrc_t r, unsigned off) { return __builtin_amdgcn_raw_buffer_load_b64(r, off, 0, 16); }
DI void cmul(float ar, float ai, float br, float bi, float& cr, float& ci) { cr = ar * br - ai * bi; ci = ar * bi + ai * br; }
template <int N> DI void wait_vm() { asm volatile("s_waitcnt vmcnt(%0)" :: "i"(N) : "memory"); }
template <int N, class T> DI void wait_vm_for(T& x) { asm volatile("s_waitcnt vmcnt(%1)" : "+v"(x) : "i"(N) : "memory"); }
template <int OFF> DI void rd128(bf16x8& d, unsigned a) { asm volatile("ds_read_b128 %0, %1 offset:%2" : "=v"(d) : "v"(a), "i"(OFF) : "memory"); }
template <int OFF> DI void rdtr(s16x4& d, unsigned a) { asm volatile("ds_read_b64_tr_b16 %0, %1 offset:%2" : "=v"(d) : "v"(a), "i"(OFF) : "memory"); }
template <int OFF, class T> DI void gld16c(T& d, const void* p) { asm volatile("global_load_dwordx4 %0, %1, off offset:%2 sc1" : "=v"(d) : "v"(p), "i"(OFF) : "memory"); }
template <class T> DI void pin(T& x) { asm volatile("" : "+v"(x) :: "memory"); }
DI void wait_lgkm0() { asm volatile("s_waitcnt lgkmcnt(0)" ::: "memory"); }
template <int... I, class F> DI void sfor_impl(std::integer_sequence<int, I...>, F&& f) { (f(std::integral_constant<int, I>{}), ...); }
template <int N, class F> DI void sfor(F&& f) { sfor_impl(std::make_integer_sequence<int, N>{}, f); }
DI void wait_flags32(const char* flags, unsigned tag, int lane) {
  GAS u64* f = (GAS u64*)flags + (lane & 31);
  const u64 want = ((u64)tag << 32) | (u64)(~tag);
  for (unsigned spins = 0; spins < SPIN_CAP; ++spins) {
    const u64 v = __hip_atomic_load(f, __ATOMIC_RELAXED, __HIP_MEMORY_SCOPE_AGENT);
    if (__all(v == want)) break;
    __builtin_amdgcn_s_sleep(4);
  }
  asm volatile("" ::: "memory");
}
DI void set_flag(char* flag, unsigned tag, int lane) {
  wait_vm<0>();
  if (lane == 0) __hip_atomic_store((GAS u64*)flag, ((u64)tag << 32) | (u64)(~tag), __ATOMIC_RELAXED, __HIP_MEMORY_SCOPE_AGENT);
}

struct Args {
  const u16 *Ax, *Lre, *Lim, *Bre, *Bim, *Cre, *Cim, *logdt, *Dsk, *Bx, *wqkv, *wo;
  char* ws;
  float* out;
};

__global__ __launch_bounds__(512, 2) void fused(Args a) {
  extern __shared__ __attribute__((aligned(16))) char smem[];
  const int tid = threadIdx.x, lane = tid & 63;
  const int w8 = __builtin_amdgcn_readfirstlane(tid >> 6), w = w8 & 3, kh = w8 >> 2;
  const int b = blockIdx.x, xcd = b & 7, idx = b >> 3, j = idx & 15;
  const int rb = 4 * (xcd >> 1) + (j >> 2), cg = 4 * (xcd & 1) + (j & 3);
  const int l15 = lane & 15, g = lane >> 4, l31 = lane & 31, hh = lane >> 5;
  const int sw = (l15 >> 1) & 7;
  const unsigned lds0 = (unsigned)(size_t)smem;
  const __amdgpu_buffer_rsrc_t wsr = mk_rsrc(a.ws, WS_SPAN);
  const bool isA = idx < 16;
  unsigned tag, tagB;
#define TAGS_READ() do { asm volatile("s_load_dword %0, %2, 0x0\n\ts_load_dword %1, %3, 0x0\n\ts_waitcnt lgkmcnt(0)" : "=&s"(tag), "=&s"(tagB) \
      : "s"(a.ws + WS_EP + (isA ? 0 : 256)), "s"(a.ws + WS_EP + 256) : "memory"); tag += 1u; tagB += 1u; } while (0)
  if (__builtin_expect(w8 >= 4, 1)) {
    const u16* src[8];
    const int rot = xcd;
    const int wstr = isA ? 64 : 64 * 1536;
#pragma unroll
    for (int i = 0; i < 4; ++i) {
      const int rho = (4 * w + i) * 8 + (lane >> 3);
      const int ch = ((lane & 7) ^ ((rho >> 1) & 7)) * 8;
      const int ii = rho & 15, m = rho >> 4;
      const int t = isA ? 32 * (ii >> 2) + 16 * (ii & 1) + 2 * m + ((ii >> 1) & 1) : rho;
      src[i] = (isA ? a.Ax : a.Bx) + (size_t)(rb * 128 + t) * 512 + ch;
      const int sidx = rho & 63;
      const int k = ((sidx >> 4) << 4) | ((sidx & 1) << 3) | ((sidx >> 1) & 7);
      const int cc = ((((lane & 7) >> 1) ^ (k & 3)) << 1) | (lane & 1);
      const u16* wa = ((rho >> 6) ? a.Bim : a.Bre) + (size_t)(cg * 64 + (rho & 63)) * 512 + ch;
      const u16* wb = a.wqkv + (size_t)k * 1536 + (1 + (rho >> 6)) * 512 + cg * 64 + cc * 8;
      src[4 + i] = isA ? wa : wb;
    }
    auto issue = [&](int s) {
      const int ks = (s + rot) & 7;
      char* base = smem + (s & 3) * 32768 + w * 4096;
#pragma unroll
      for (int i = 0; i < 4; ++i) glds16(src[i] + ks * 64, base + i * 1024);
#pragma unroll
      for (int i = 0; i < 4; ++i) glds16(src[4 + i] + ks * wstr, base + 16384 + i * 1024);
    };
#pragma clang loop unroll(disable)
    for (int s = 0; s < 2; ++s) issue(s);
    TAGS_READ();
    wait_vm<8>();
    BAR();
    issue(2);
#pragma clang loop unroll(disable)
    for (int k = 0; k < 5; ++k) {
      if (k) { wait_vm<16>(); BAR(); }
      issue(k + 3);
    }
    if (isA) {
      const u16* cs[2];
#pragma unroll
      for (int i = 0; i < 2; ++i) {
        const int rho = (2 * w + i) * 8 + (lane >> 3);
        cs[i] = a.Cre + (size_t)(cg * 64 + rho) * 512 + ((lane & 7) ^ ((rho >> 1) & 7)) * 8;
      }
      const ptrdiff_t imo = a.Cim - a.Cre;
      wait_vm<16>(); BAR();
      wait_vm<8>(); BAR();
      wait_vm<0>(); BAR();
      BAR();
      if (w == 0) {
        const int h = cg * 4 + (rb & 3), c = rb >> 2;
        u32x4 v[16];
        for (unsigned spins = 0; spins < SPIN_CAP; ++spins) {
          bool ok = true;
          asm volatile("" ::: "memory");
#pragma unroll
          for (int i = 0; i < 16; ++i) v[i] = ld16c(wsr, WS_KVS + (((i * 32 + h) * 4 + c) * 64 + lane) * 16);
          SB0;
#pragma unroll
          for (int i = 0; i < 16; ++i) ok &= (v[i][1] == tagB) & (v[i][3] == tagB);
          if (__all(ok)) break;
          __builtin_amdgcn_s_sleep(2);
        }
        float s0 = 0.f, s1 = 0.f;
#pragma unroll
        for (int i = 0; i < 16; ++i) { s0 += __uint_as_float(v[i][0]); s1 += __uint_as_float(v[i][2]); }
        st8(wsr, WS_KVF + ((h * 4 + c) * 64 + lane) * 8, (u32x2){pk2(s0, s1), tagB});
      }
      {
        LAS u64* lf = (LAS u64*)smem + w * 256;
        const u64 want = ((u64)tag << 32) | (u64)(~tag);
        for (unsigned spins = 0; spins < SPIN_CAP; ++spins) {
          if (__hip_atomic_load(lf, __ATOMIC_RELAXED, __HIP_MEMORY_SCOPE_WORKGROUP) == want) break;
          __builtin_amdgcn_s_sleep(2);
        }
        asm volatile("" ::: "memory");
      }
#pragma clang loop unroll(disable)
      for (int sg = 15; sg >= 0; --sg) {
#pragma unroll
        for (int i = 0; i < 2; ++i) glds16(cs[i] + (sg >> 3) * imo + (sg & 7) * 64, smem + sg * 8192 + (2 * w + i) * 1024);
      }
    } else {
      const u16* qs[2];
#pragma unroll
      for (int i = 0; i < 2; ++i) {
        const int sidx = (w * 2 + i) * 8 + (lane >> 3);
        const int k = ((sidx >> 4) << 4) | ((sidx & 1) << 3) | ((sidx >> 1) & 7);
        const int cc = ((((lane & 7) >> 1) ^ (k & 3)) << 1) | (lane & 1);
        qs[i] = a.wqkv + (size_t)k * 1536 + cg * 64 + cc * 8;
      }
      auto issueq = [&](int s) {
        const int ks = (s + rot) & 7;
        char* base = smem + (s & 3) * 32768;
#pragma unroll
        for (int i = 0; i < 4; ++i) glds16(src[i] + ks * 64, base + w * 4096 + i * 1024);
#pragma unroll
        for (int i = 0; i < 2; ++i) glds16(qs[i] + ks * (64 * 1536), base + 16384 + w * 2048 + i * 1024);
      };
      const int k0 = w * 8 + (lane >> 3);
      const u16* os = a.wo + (size_t)k0 * 512 + cg * 64 + ((lane & 7) ^ (((k0 >> 1) & 1) << 2)) * 8;
#pragma clang loop unroll(disable)
      for (int k = 5; k < 13; ++k) {
        if (k == 5) wait_vm<16>(); else if (k == 6) wait_vm<14>(); else if (k >= 9 && k <= 11) wait_vm<28>(); else wait_vm<12>();
        BAR();
        issueq(k + 3);
        if (k == 8) {
#pragma clang loop unroll(disable)
          for (int i = 0; i < 16; ++i) {
            const int il = i & 7;
            const int dst = il < 4 ? (i >> 3) * 65536 + (il >> 1) * 32768 + 24576 + (il & 1) * 4096 : 131072 + (i >> 3) * 16384 + (il - 4) * 4096;
            glds16(os + (size_t)i * 32 * 512, smem + dst + w * 1024);
          }
        }
      }
      wait_vm<12>(); BAR();
      wait_vm<6>(); BAR();
      wait_vm<0>(); BAR();
      BAR();
    }
    wait_vm<0>();
  } else if (isA) {
    const int p = cg * 64 + w * 16 + l15;
    const float la = __uint_as_float((unsigned)a.Lre[p] << 16), lb = __uint_as_float((unsigned)a.Lim[p] << 16);
    const float dt = bfr(expf(__uint_as_float((unsigned)a.logdt[p] << 16)));
    float Lr, Li;
    { const float er = expf(dt * la); float sn, cs; sincosf(dt * lb, &sn, &cs); Lr = er * cs; Li = er * sn; }
    float c2r, c2i;
    { const float nr = Lr - 1.f, ni = Li, den = la * la + lb * lb;
      c2r = 2.f * ((nr * la + ni * lb) / den); c2i = 2.f * ((ni * la - nr * lb) / den); }
    float L16r, L16i, L32r, L32i, L64r, L64i, L128r, L128i, L512r, L512i, Lgr, Lgi;
    { float tr, ti, ur, ui;
      cmul(Lr, Li, Lr, Li, tr, ti); cmul(tr, ti, tr, ti, ur, ui); cmul(ur, ui, ur, ui, tr, ti); cmul(tr, ti, tr, ti, L16r, L16i);
      cmul(L16r, L16i, L16r, L16i, L32r, L32i); cmul(L32r, L32i, L32r, L32i, L64r, L64i);
      cmul(L64r, L64i, L64r, L64i, L128r, L128i); cmul(L128r, L128i, L128r, L128i, tr, ti); cmul(tr, ti, tr, ti, L512r, L512i);
      cmul(L32r, L32i, L64r, L64i, tr, ti);
      Lgr = g == 0 ? 1.f : g == 1 ? L32r : g == 2 ? L64r : tr;
      Lgi = g == 0 ? 0.f : g == 1 ? L32i : g == 2 ? L64i : ti; }
    asm volatile("" : "+v"(Lr), "+v"(Li), "+v"(c2r), "+v"(c2i), "+v"(L16r), "+v"(L16i), "+v"(L32r), "+v"(L32i), "+v"(L64r), "+v"(L64i),
                 "+v"(L128r), "+v"(L128i), "+v"(L512r), "+v"(L512i), "+v"(Lgr), "+v"(Lgi));
    f32x4 acc[8][2];
#pragma unroll
    for (int m = 0; m < 8; ++m) { acc[m][0] = (f32x4){0.f, 0.f, 0.f, 0.f}; acc[m][1] = (f32x4){0.f, 0.f, 0.f, 0.f}; }
    const unsigned xoff = lds0 + l15 * 128 + ((g ^ sw) * 16);
    const unsigned woff = lds0 + 16384 + (w * 16 + l15) * 128 + ((g ^ sw) * 16);
    bf16x8 X[2][8], W[2][2];
    auto rdA = [&](auto rc, auto kc, unsigned so) {
      constexpr int r = decltype(rc)::value, kk = decltype(kc)::value;
      if constexpr (r < 2) rd128<r * 8192>(W[kk][r], (woff ^ (kk * 64)) + so);
      else rd128<(r - 2) * 2048>(X[kk][r - 2], (xoff ^ (kk * 64)) + so);
    };
    TAGS_READ();
    BAR();
    sfor<10>([&](auto rc) { rdA(rc, std::integral_constant<int, 0>{}, 0u); });
    wait_lgkm0();
#pragma clang loop unroll(disable)
    for (int s = 0; s < 8; ++s) {
      sfor<2>([&](auto uc) {
        constexpr int u = decltype(uc)::value;
        const unsigned so = ((s + u) & 3) * 32768;
        sfor<8>([&](auto i) { pin(X[u][decltype(i)::value]); });
        sfor<2>([&](auto i) { pin(W[u][decltype(i)::value]); });
        if constexpr (u == 1) BAR();
        sfor<4>([&](auto gc) {
          constexpr int G = decltype(gc)::value;
          constexpr int r0 = G < 2 ? 3 * G : 6 + 2 * (G - 2), nr = G < 2 ? 3 : 2;
          sfor<nr>([&](auto rr) { rdA(std::integral_constant<int, r0 + decltype(rr)::value>{}, std::integral_constant<int, u ^ 1>{}, so); });
#pragma unroll
          for (int mm = 0; mm < 2; ++mm) {
            acc[2 * G + mm][0] = MFMA16(X[u][2 * G + mm], W[u][0], acc[2 * G + mm][0]);
            acc[2 * G + mm][1] = MFMA16(X[u][2 * G + mm], W[u][1], acc[2 * G + mm][1]);
          }
          SB0;
        });
        wait_lgkm0();
      });
    }
    const f32x2 LR = {Lr, Lr}, LI = {Li, Li}, NLI = {-Li, -Li};
    f32x2 AR = {0.f, 0.f}, AI = {0.f, 0.f};
#pragma unroll
    for (int m = 0; m < 8; ++m)
#pragma unroll
      for (int r2 = 0; r2 < 2; ++r2) {
        const f32x2 UR = {acc[m][0][2 * r2], acc[m][0][2 * r2 + 1]}, UI = {acc[m][1][2 * r2], acc[m][1][2 * r2 + 1]};
        const f32x2 nr = __builtin_elementwise_fma(LR, AR, __builtin_elementwise_fma(NLI, AI, UR));
        const f32x2 ni = __builtin_elementwise_fma(LR, AI, __builtin_elementwise_fma(LI, AR, UI));
        AR = nr; AI = ni;
      }
    float ar, ai;
    cmul(L16r, L16i, AR[0], AI[0], ar, ai); ar += AR[1]; ai += AI[1];
    {
      float yr = __shfl_up(ar, 16), yi = __shfl_up(ai, 16), tr, ti;
      cmul(L32r, L32i, yr, yi, tr, ti);
      if (g >= 1) { ar += tr; ai += ti; }
      yr = __shfl_up(ar, 32); yi = __shfl_up(ai, 32);
      cmul(L64r, L64i, yr, yi, tr, ti);
      if (g >= 2) { ar += tr; ai += ti; }
    }
    if (g == 3) st16(wsr, WS_AGG + (rb * 512 + p) * 16, (u32x4){__float_as_uint(ar), tag, __float_as_uint(ai), tag});
    float er_ = __shfl_up(ar, 16), ei_ = __shfl_up(ai, 16);
    if (g == 0) { er_ = 0.f; ei_ = 0.f; }
    if (rb > 0) {
      u32x4 v[4];
      const int j0 = rb - 1 - 4 * g;
      for (unsigned spins = 0; spins < SPIN_CAP; ++spins) {
        bool ok = true;
        asm volatile("" ::: "memory");
#pragma unroll
        for (int i = 0; i < 4; ++i) { const int jj = j0 - i; v[i] = ld16c(wsr, WS_AGG + ((jj < 0 ? 0 : jj) * 512 + p) * 16); }
        SB0;
#pragma unroll
        for (int i = 0; i < 4; ++i) ok &= ((j0 - i) < 0) | ((v[i][1] == tag) & (v[i][3] == tag));
        if (__all(ok)) break;
        __builtin_amdgcn_s_sleep(1);
      }
      float qr = 0.f, qi = 0.f;
#pragma unroll
      for (int i = 3; i >= 0; --i) {
        const float vr = __uint_as_float(v[i][0]), vi = __uint_as_float(v[i][2]);
        const bool valid = (j0 - i) >= 0, first = (i == 3 || (j0 - i - 1) < 0);
        const float nr = fmaf(L128r, qr, fmaf(-L128i, qi, vr)), ni = fmaf(L128r, qi, fmaf(L128i, qr, vi));
        qr = valid ? (first ? vr : nr) : 0.f; qi = valid ? (first ? vi : ni) : 0.f;
      }
      const float q0r = __shfl(qr, l15), q0i = __shfl(qi, l15), q1r = __shfl(qr, l15 + 16), q1i = __shfl(qi, l15 + 16);
      const float q2r = __shfl(qr, l15 + 32), q2i = __shfl(qi, l15 + 32), q3r = __shfl(qr, l15 + 48), q3i = __shfl(qi, l15 + 48);
      float tr = q3r, ti = q3i, ur, ui;
      if (rb > 12) { cmul(L512r, L512i, tr, ti, ur, ui); tr = q2r + ur; ti = q2i + ui; } else { tr = q2r; ti = q2i; }
      if (rb > 8)  { cmul(L512r, L512i, tr, ti, ur, ui); tr = q1r + ur; ti = q1i + ui; } else { tr = q1r; ti = q1i; }
      if (rb > 4)  { cmul(L512r, L512i, tr, ti, ur, ui); tr = q0r + ur; ti = q0i + ui; } else { tr = q0r; ti = q0i; }
      cmul(Lgr, Lgi, tr, ti, ur, ui);
      er_ += ur; ei_ += ui;
    }
    if (lane == 0) __hip_atomic_store((LAS u64*)smem + w * 256, ((u64)tag << 32) | (u64)(~tag), __ATOMIC_RELAXED, __HIP_MEMORY_SCOPE_WORKGROUP);
    f32x2 HR, HI;
    { float tr, ti; cmul(L16r, L16i, er_, ei_, tr, ti); HR = (f32x2){er_, tr + AR[0]}; HI = (f32x2){ei_, ti + AI[0]}; }
    const f32x2 CR = {c2r, c2r}, NCI = {-c2i, -c2i}, NCR = {-c2r, -c2r};
    u32x4 o_re[2][2], o_im[2][2];
#pragma unroll
    for (int m = 0; m < 8; ++m) {
      f32x2 ore[2], oim[2];
#pragma unroll
      for (int r2 = 0; r2 < 2; ++r2) {
        const f32x2 UR = {acc[m][0][2 * r2], acc[m][0][2 * r2 + 1]}, UI = {acc[m][1][2 * r2], acc[m][1][2 * r2 + 1]};
        const f32x2 nr = __builtin_elementwise_fma(LR, HR, __builtin_elementwise_fma(NLI, HI, UR));
        const f32x2 ni = __builtin_elementwise_fma(LR, HI, __builtin_elementwise_fma(LI, HR, UI));
        HR = nr; HI = ni;
        ore[r2] = __builtin_elementwise_fma(CR, HR, NCI * HI);
        oim[r2] = __builtin_elementwise_fma(NCR, HI, NCI * HR);
      }
      o_re[0][m >> 2][m & 3] = pk2(ore[0][0], ore[1][0]); o_re[1][m >> 2][m & 3] = pk2(ore[0][1], ore[1][1]);
      o_im[0][m >> 2][m & 3] = pk2(oim[0][0], oim[1][0]); o_im[1][m >> 2][m & 3] = pk2(oim[0][1], oim[1][1]);
    }
    char* tb = smem + 131072 + w * 8192;
#pragma unroll
    for (int c = 0; c < 2; ++c)
#pragma unroll
      for (int hf = 0; hf < 2; ++hf) {
        *(u32x4*)(tb + l15 * 256 + ((g ^ (l15 & 3)) * 64) + c * 32 + hf * 16) = o_re[c][hf];
        *(u32x4*)(tb + 4096 + l15 * 256 + ((g ^ (l15 & 3)) * 64) + c * 32 + hf * 16) = o_im[c][hf];
      }
    __builtin_amdgcn_fence(__ATOMIC_RELEASE, "wavefront");
    __builtin_amdgcn_wave_barrier();
    {
      const int q = (lane >> 2) & 3, pp = lane & 3;
      const char* rbase = tb + (8 * hh + q) * 256 + (16 * (g & 1) + 4 * pp) * 2;
#pragma unroll
      for (int part = 0; part < 2; ++part)
#pragma unroll
        for (int tt = 0; tt < 4; ++tt) {
          const s16x4 lo = tr_read(rbase + part * 4096 + ((tt ^ q) * 64)), hi4 = tr_read(rbase + part * 4096 + ((tt ^ q) * 64) + 4 * 256);
          const bf16x8 f = __builtin_shufflevector(lo, hi4, 0, 1, 2, 3, 4, 5, 6, 7);
          st16(wsr, WS_HF + (((rb * 4 + tt) * 64 + part * 32 + cg * 4 + w) * 64 + lane) * 16, __builtin_bit_cast(u32x4, f));
        }
    }
    set_flag(a.ws + WS_FH + (rb * 32 + cg * 4 + w) * 8, tag, lane);
  } else {
    const unsigned xoff = lds0 + l15 * 128 + ((g ^ sw) * 16);
    const int q = (lane >> 2) & 3, pp = lane & 3;
    const unsigned woff = lds0 + 16384 + ((((g >> 1) * 8 + q) * 2 + (g & 1)) * 128) + ((w ^ q) * 32) + pp * 8;
    const int h = cg * 4 + w;
    bf16x8 X[2][8]; s16x4 T[2][4];
    auto rdB = [&](auto rc, auto kc, auto nc, unsigned so) {
      constexpr int r = decltype(rc)::value, kk = decltype(kc)::value, NT = decltype(nc)::value;
      if constexpr (r < NT) rdtr<(r >> 1) * 8192 + kk * 4096 + (r & 1) * 1024>(T[kk][r], woff + so);
      else if constexpr (r < NT + 8) rd128<(r - NT) * 2048>(X[kk][r - NT], (xoff ^ (kk * 64)) + so);
    };
    using I2 = std::integral_constant<int, 2>; using I4 = std::integral_constant<int, 4>;
    {
      f32x4 ak[8], av[8];
#pragma unroll
      for (int m = 0; m < 8; ++m) { ak[m] = (f32x4){0.f, 0.f, 0.f, 0.f}; av[m] = ak[m]; }
      TAGS_READ();
      BAR();
      sfor<12>([&](auto rc) { rdB(rc, std::integral_constant<int, 0>{}, I4{}, 0u); });
      wait_lgkm0();
#pragma clang loop unroll(disable)
      for (int s = 0; s < 8; ++s) {
        sfor<2>([&](auto uc) {
          constexpr int u = decltype(uc)::value;
          const unsigned so = ((s + u) & 3) * 32768;
          sfor<8>([&](auto i) { pin(X[u][decltype(i)::value]); });
          sfor<4>([&](auto i) { pin(T[u][decltype(i)::value]); });
          if constexpr (u == 1) BAR();
          const bf16x8 wk = __builtin_shufflevector(T[u][0], T[u][1], 0, 1, 2, 3, 4, 5, 6, 7);
          const bf16x8 wv = __builtin_shufflevector(T[u][2], T[u][3], 0, 1, 2, 3, 4, 5, 6, 7);
          sfor<4>([&](auto gc) {
            constexpr int G = decltype(gc)::value;
            sfor<3>([&](auto rr) { rdB(std::integral_constant<int, 3 * G + decltype(rr)::value>{}, std::integral_constant<int, u ^ 1>{}, I4{}, so); });
#pragma unroll
            for (int mm = 0; mm < 2; ++mm) {
              ak[2 * G + mm] = MFMA16(X[u][2 * G + mm], wk, ak[2 * G + mm]);
              av[2 * G + mm] = MFMA16(X[u][2 * G + mm], wv, av[2 * G + mm]);
            }
            SB0;
          });
          wait_lgkm0();
        });
      }
      f32x4 kvp = {0.f, 0.f, 0.f, 0.f}, zp = {0.f, 0.f, 0.f, 0.f};
      const bf16x8 ones = {0x3F80, 0x3F80, 0x3F80, 0x3F80, 0x3F80, 0x3F80, 0x3F80, 0x3F80};
#pragma unroll
      for (int s2 = 0; s2 < 4; ++s2) {
        u32x4 pkf, vf;
        pkf[0] = phi2(ak[2 * s2][0], ak[2 * s2][1]); pkf[1] = phi2(ak[2 * s2][2], ak[2 * s2][3]);
        pkf[2] = phi2(ak[2 * s2 + 1][0], ak[2 * s2 + 1][1]); pkf[3] = phi2(ak[2 * s2 + 1][2], ak[2 * s2 + 1][3]);
        vf[0] = pk2(av[2 * s2][0], av[2 * s2][1]); vf[1] = pk2(av[2 * s2][2], av[2 * s2][3]);
        vf[2] = pk2(av[2 * s2 + 1][0], av[2 * s2 + 1][1]); vf[3] = pk2(av[2 * s2 + 1][2], av[2 * s2 + 1][3]);
        const bf16x8 pb = __builtin_bit_cast(bf16x8, pkf), vb = __builtin_bit_cast(bf16x8, vf);
        kvp = MFMA16(pb, vb, kvp);
        zp = MFMA16(pb, ones, zp);
      }
      const unsigned so = WS_KVS + ((rb * 32 + h) * 4 * 64 + lane) * 16;
      st16(wsr, so, (u32x4){__float_as_uint(kvp[0]), tag, __float_as_uint(kvp[1]), tag});
      st16(wsr, so + 1024, (u32x4){__float_as_uint(kvp[2]), tag, __float_as_uint(kvp[3]), tag});
      st16(wsr, so + 2048, (u32x4){__float_as_uint(zp[0]), tag, __float_as_uint(zp[1]), tag});
      st16(wsr, so + 3072, (u32x4){__float_as_uint(zp[2]), tag, __float_as_uint(zp[3]), tag});
    }
    f32x4 aq[8];
#pragma unroll
    for (int m = 0; m < 8; ++m) aq[m] = (f32x4){0.f, 0.f, 0.f, 0.f};
#pragma clang loop unroll(disable)
    for (int s = 8; s < 16; ++s) {
      sfor<2>([&](auto uc) {
        constexpr int u = decltype(uc)::value;
        const unsigned so = ((s + u) & 3) * 32768;
        sfor<8>([&](auto i) { pin(X[u][decltype(i)::value]); });
        sfor<2>([&](auto i) { pin(T[u][decltype(i)::value]); });
        if constexpr (u == 1) BAR();
        const bf16x8 wq = __builtin_shufflevector(T[u][0], T[u][1], 0, 1, 2, 3, 4, 5, 6, 7);
        sfor<4>([&](auto gc) {
          constexpr int G = decltype(gc)::value;
          sfor<3>([&](auto rr) { rdB(std::integral_constant<int, 3 * G + decltype(rr)::value>{}, std::integral_constant<int, u ^ 1>{}, I2{}, so); });
          aq[2 * G] = MFMA16(wq, X[u][2 * G], aq[2 * G]);
          aq[2 * G + 1] = MFMA16(wq, X[u][2 * G + 1], aq[2 * G + 1]);
          SB0;
        });
        wait_lgkm0();
      });
    }
    u32x2 pq[8];
#pragma unroll
    for (int m = 0; m < 8; ++m) { pq[m][0] = phi2(aq[m][0], aq[m][1]); pq[m][1] = phi2(aq[m][2], aq[m][3]); }
    u32x2 kf[4];
    for (unsigned spins = 0; spins < SPIN_CAP; ++spins) {
      bool ok = true;
      asm volatile("" ::: "memory");
#pragma unroll
      for (int i = 0; i < 4; ++i) kf[i] = ld8c(wsr, WS_KVF + ((h * 4 + i) * 64 + lane) * 8);
#pragma unroll
      for (int i = 0; i < 4; ++i) ok &= (kf[i][1] == tag);
      if (__all(ok)) break;
      __builtin_amdgcn_s_sleep(2);
    }
    const s16x4 kvA = __builtin_bit_cast(s16x4, (u32x2){kf[0][0], kf[1][0]}), zA = __builtin_bit_cast(s16x4, (u32x2){kf[2][0], kf[3][0]});
#pragma unroll
    for (int m = 0; m < 8; ++m) {
      const s16x4 pb = __builtin_bit_cast(s16x4, pq[m]);
      const f32x4 z4 = {0.f, 0.f, 0.f, 0.f};
      const f32x4 num = __builtin_amdgcn_mfma_f32_16x16x16bf16_1k(kvA, pb, z4, 0, 0, 0);
      const f32x4 dn = __builtin_amdgcn_mfma_f32_16x16x16bf16_1k(zA, pb, z4, 0, 0, 0);
      const float rc = __builtin_amdgcn_rcpf(bfr(bfr(dn[0]) + 1e-6f));
      u32x2 o;
      o[0] = pk2(bfr(num[0]) * rc, bfr(num[1]) * rc); o[1] = pk2(bfr(num[2]) * rc, bfr(num[3]) * rc);
      const int tt = rb * 4 + (m >> 1), t31 = (m & 1) * 16 + l15;
      st8(wsr, WS_PQ + ((tt * 32 + h) * 64 + t31 + 32 * (g >> 1)) * 16 + (g & 1) * 8, o);
    }
    set_flag(a.ws + WS_FQ + (rb * 32 + h) * 8, tag, lane);
  }

  const int d4 = lane & 15, r4 = lane >> 4;
  const __amdgpu_buffer_rsrc_t outr = mk_rsrc(a.out, 2048 * 1024 * 4);
  f32x16 acc[2];
#pragma unroll
  for (int r = 0; r < 16; ++r) { acc[0][r] = 0.f; acc[1][r] = 0.f; }
  u32x2 xv[4], dv;
  if (isA) {
    {
      const u16* xp = a.Ax + (size_t)(rb * 128 + w8 * 16 + r4) * 512 + cg * 64 + d4 * 4;
      asm volatile("global_load_dwordx2 %0, %5, off offset:-4096\n\tglobal_load_dwordx2 %1, %5, off\n\t"
                   "global_load_dwordx2 %2, %6, off offset:-4096\n\tglobal_load_dwordx2 %3, %6, off\n\t"
                   "global_load_dwordx2 %4, %7, off"
                   : "=&v"(xv[0]), "=&v"(xv[1]), "=&v"(xv[2]), "=&v"(xv[3]), "=&v"(dv)
                   : "v"(xp + 2048), "v"(xp + 2048 * 3), "v"(a.Dsk + cg * 64 + d4 * 4) : "memory");
    }
    if (w8 == 0) {
      wait_flags32(a.ws + WS_FH + rb * 256, tag, lane);
      if (rb == 15 && cg == 0 && lane == 0)
        __hip_atomic_store((GAS unsigned*)(a.ws + WS_EP), tag, __ATOMIC_RELAXED, __HIP_MEMORY_SCOPE_AGENT);
    }
    BAR();
    const char* hf = a.ws + WS_HF + (((size_t)(rb * 4 + w) * 64 + kh * 32) * 64 + lane) * 16;
    bf16x8 H[32];
    sfor<32>([&](auto i) { constexpr int k = decltype(i)::value; gld16c<(k & 3) * 1024>(H[k], hf + (k >> 2) * 4096); });
    unsigned cb[4];
#pragma unroll
    for (int i = 0; i < 4; ++i) cb[i] = lds0 + kh * 65536 + l31 * 128 + (((2 * i + hh) ^ ((l31 >> 1) & 7)) * 16);
    bf16x8 Cc[2][2];
    rd128<0>(Cc[0][0], cb[0]); rd128<4096>(Cc[0][1], cb[0]);
    sfor<32>([&](auto kc) {
      constexpr int k = decltype(kc)::value, u = k & 1;
      wait_vm_for<31 - k>(H[k]);
      if constexpr (k < 31) {
        rd128<((k + 1) >> 2) * 8192>(Cc[u ^ 1][0], cb[(k + 1) & 3]); rd128<((k + 1) >> 2) * 8192 + 4096>(Cc[u ^ 1][1], cb[(k + 1) & 3]);
        asm volatile("s_waitcnt lgkmcnt(2)" : "+v"(Cc[u][0]), "+v"(Cc[u][1]) :: "memory");
      } else {
        asm volatile("s_waitcnt lgkmcnt(0)" : "+v"(Cc[u][0]), "+v"(Cc[u][1]) :: "memory");
      }
      acc[0] = MFMA32(Cc[u][0], H[k], acc[0]);
      acc[1] = MFMA32(Cc[u][1], H[k], acc[1]);
      SB0;
    });
  } else {
    if (w8 == 0) wait_flags32(a.ws + WS_FQ + rb * 256, tag, lane);
    BAR();
    if (rb == 0 && cg == 0 && tid == 0)
      __hip_atomic_store((GAS unsigned*)(a.ws + WS_EP + 256), tag, __ATOMIC_RELAXED, __HIP_MEMORY_SCOPE_AGENT);
    const char* pqf = a.ws + WS_PQ + (((size_t)(rb * 4 + w) * 32 + kh * 16) * 64 + lane) * 16;
    bf16x8 P[16];
    sfor<16>([&](auto i) { constexpr int k = decltype(i)::value; gld16c<(k & 3) * 1024>(P[k], pqf + (k >> 2) * 4096); });
    const int q = (lane >> 2) & 3, pp = lane & 3;
    const unsigned wl = (8 * hh + q) * 128 + ((((g & 1) * 2 + (pp >> 1)) ^ ((q >> 1) << 2)) * 16) + (pp & 1) * 8;
    const unsigned wT0 = lds0 + kh * 65536 + 24576 + wl, wT1 = wT0 ^ 64u, wU0 = lds0 + 131072 + kh * 16384 + wl, wU1 = wU0 ^ 64u;
    s16x4 Wt[2][4];
    auto rdW = [&](auto hc) {
      constexpr int h = decltype(hc)::value, u = h & 1;
      constexpr int o = h < 8 ? (h >> 2) * 32768 + (h & 3) * 2048 : (h - 8) * 2048;
      rdtr<o>(Wt[u][0], h < 8 ? wT0 : wU0); rdtr<o + 512>(Wt[u][1], h < 8 ? wT0 : wU0);
      rdtr<o>(Wt[u][2], h < 8 ? wT1 : wU1); rdtr<o + 512>(Wt[u][3], h < 8 ? wT1 : wU1);
    };
    rdW(std::integral_constant<int, 0>{});
    sfor<16>([&](auto hc) {
      constexpr int h = decltype(hc)::value, u = h & 1;
      wait_vm_for<15 - h>(P[h]);
      if constexpr (h < 15) {
        rdW(std::integral_constant<int, h + 1>{});
        asm volatile("s_waitcnt lgkmcnt(4)" : "+v"(Wt[u][0]), "+v"(Wt[u][1]), "+v"(Wt[u][2]), "+v"(Wt[u][3]) :: "memory");
      } else {
        asm volatile("s_waitcnt lgkmcnt(0)" : "+v"(Wt[u][0]), "+v"(Wt[u][1]), "+v"(Wt[u][2]), "+v"(Wt[u][3]) :: "memory");
      }
      acc[0] = MFMA32(__builtin_shufflevector(Wt[u][0], Wt[u][1], 0, 1, 2, 3, 4, 5, 6, 7), P[h], acc[0]);
      acc[1] = MFMA32(__builtin_shufflevector(Wt[u][2], Wt[u][3], 0, 1, 2, 3, 4, 5, 6, 7), P[h], acc[1]);
      SB0;
    });
  }
  BAR();
  {
    char* tb = smem + kh * 32768 + w * 8192;
#pragma unroll
    for (int nt = 0; nt < 2; ++nt)
#pragma unroll
      for (int q = 0; q < 4; ++q)
        *(f32x4*)(tb + l31 * 256 + (((nt * 8 + 2 * q + hh) ^ (l31 & 15)) * 16)) =
            (f32x4){acc[nt][4 * q], acc[nt][4 * q + 1], acc[nt][4 * q + 2], acc[nt][4 * q + 3]};
  }
  wait_lgkm0();
  BAR();
  if (isA) { sfor<4>([&](auto i) { pin(xv[decltype(i)::value]); }); pin(dv); }
#pragma unroll
  for (int it = 0; it < 4; ++it) {
    const int row = w8 * 16 + it * 4 + r4;
    const char* sp = smem + row * 256 + ((d4 ^ (row & 15)) * 16);
    f32x4 s = *(const f32x4*)sp + *(const f32x4*)(sp + 32768);
    const unsigned oo = ((rb * 128 + row) * 1024 + cg * 64 + d4 * 4) * 4;
    if (isA) {
      s[0] += bfr(__uint_as_float(xv[it][0] << 16) * __uint_as_float(dv[0] << 16));
      s[1] += bfr(__uint_as_float(xv[it][0] & 0xffff0000u) * __uint_as_float(dv[0] & 0xffff0000u));
      s[2] += bfr(__uint_as_float(xv[it][1] << 16) * __uint_as_float(dv[1] << 16));
      s[3] += bfr(__uint_as_float(xv[it][1] & 0xffff0000u) * __uint_as_float(dv[1] & 0xffff0000u));
      st16(outr, oo, __builtin_bit_cast(u32x4, s));
    } else {
      s[0] = bfr(s[0]); s[1] = bfr(s[1]); s[2] = bfr(s[2]); s[3] = bfr(s[3]);
      st16(outr, oo + 2048, __builtin_bit_cast(u32x4, s));
    }
  }
}

extern "C" void kernel_launch(void* const* d_in, const int* in_sizes, int n_in, void* d_out, int out_size,
                              void* d_ws, size_t ws_size, hipStream_t stream) {
  Args a;
  a.Ax = (const u16*)d_in[0]; a.Lre = (const u16*)d_in[1]; a.Lim = (const u16*)d_in[2];
  a.Bre = (const u16*)d_in[3]; a.Bim = (const u16*)d_in[4]; a.Cre = (const u16*)d_in[5]; a.Cim = (const u16*)d_in[6];
  a.logdt = (const u16*)d_in[7]; a.Dsk = (const u16*)d_in[8]; a.Bx = (const u16*)d_in[9];
  a.wqkv = (const u16*)d_in[10]; a.wo = (const u16*)d_in[11];
  a.ws = (char*)d_ws; a.out = (float*)d_out;
  (void)hipFuncSetAttribute((const void*)fused, hipFuncAttributeMaxDynamicSharedMemorySize, LDS_BYTES);
  fused<<<256, 512, LDS_BYTES, stream>>>(a);
}
```
